```python
import torch

def _softmax(x, axis=-1):
    return torch.softmax(x, dim=axis)
def _gelu(x):
    return torch.nn.functional.gelu(x, approximate="tanh")
def _silu(x):
    return torch.nn.functional.silu(x)
def _sigmoid(x):
    return torch.sigmoid(x)
def _layernorm(x, axis=-1, eps=1e-5):
    m = x.mean(dim=axis, keepdim=True)
    v = x.var(dim=axis, keepdim=True, unbiased=False)
    return (x - m) / torch.sqrt(v + eps)
def _scatter_add(tgt, idx, src, axis=0):
    return tgt.clone().index_add_(axis, idx, src)
def _segment_sum(src, seg, n):
    out = torch.zeros((n,) + src.shape[1:], dtype=src.dtype)
    return out.index_add_(0, seg, src)
def _pad(x, pw):
    flat = tuple(v for pair in reversed(pw) for v in pair)
    return torch.nn.functional.pad(x, flat)

kernel_name = 'standalone__rmsnorm__sampling'

def setup_inputs():

    _w = (lambda a: (lambda t: t.to(torch.bfloat16) if t.is_floating_point() else t)(torch.as_tensor(a)))
    B, S, D = 1024, 16, 448
    return {
        "x": _w((lambda *s: torch.randn(*s, dtype=torch.bfloat16) * 0.05)(B, S, D)),
        "g": _w((lambda *s: torch.randn(*s, dtype=torch.bfloat16) * 0.05)(D) + 1.0),
    }

def reference(x, g):
    rms = torch.sqrt((x * x).mean(axis=-1, keepdims=True) + 1e-6)
    return x / rms * g
```

```cpp
#include <hip/hip_runtime.h>
#include <hip/hip_bf16.h>
#include <stdint.h>

#define ROWS 4
#define TPB 256

typedef unsigned int u32x4 __attribute__((ext_vector_type(4)));
typedef float f32x2 __attribute__((ext_vector_type(2)));
typedef __bf16 bf16x2 __attribute__((ext_vector_type(2)));

constexpr int D = 448;
constexpr int CH = D / 8;

__device__ __forceinline__ float lo_f(unsigned w) { return __uint_as_float(w << 16); }
__device__ __forceinline__ float hi_f(unsigned w) { return __uint_as_float(w & 0xffff0000u); }

__device__ __forceinline__ unsigned pack_bf16(float a, float b) {
    f32x2 v = {a, b};
    bf16x2 r = __builtin_convertvector(v, bf16x2);
    return __builtin_bit_cast(unsigned, r);
}

template <int CTRL>
__device__ __forceinline__ float dpp_add(float v) {
    int t = __builtin_amdgcn_update_dpp(0, __float_as_int(v), CTRL, 0xf, 0xf, true);
    return v + __int_as_float(t);
}

__device__ __forceinline__ float wave_sum(float v) {
    v = dpp_add<0xB1>(v);
    v = dpp_add<0x4E>(v);
    v = dpp_add<0x141>(v);
    v = dpp_add<0x140>(v);
    float s = __int_as_float(__builtin_amdgcn_readlane(__float_as_int(v), 0)) +
              __int_as_float(__builtin_amdgcn_readlane(__float_as_int(v), 16)) +
              __int_as_float(__builtin_amdgcn_readlane(__float_as_int(v), 32)) +
              __int_as_float(__builtin_amdgcn_readlane(__float_as_int(v), 48));
    return s;
}

__global__ __launch_bounds__(TPB) void rmsnorm_kernel(const u32x4* __restrict__ x, const u32x4* __restrict__ g,
                                                      u32x4* __restrict__ out, int nrows) {
    const int lane = threadIdx.x & 63;
    const int wave = (blockIdx.x * (TPB / 64)) + (threadIdx.x >> 6);
    const int row0 = wave * ROWS;
    if (lane >= CH) return;
    u32x4 v[ROWS];
#pragma unroll
    for (int r = 0; r < ROWS; ++r) {
        v[r] = __builtin_nontemporal_load(&x[(size_t)(row0 + r) * CH + lane]);
    }
    u32x4 gv = g[lane];
    float gf[8];
#pragma unroll
    for (int j = 0; j < 4; ++j) { gf[2 * j] = lo_f(gv[j]); gf[2 * j + 1] = hi_f(gv[j]); }
#pragma unroll
    for (int r = 0; r < ROWS; ++r) {
        float f[8];
        float ss = 0.f;
#pragma unroll
        for (int j = 0; j < 4; ++j) {
            f[2 * j] = lo_f(v[r][j]); f[2 * j + 1] = hi_f(v[r][j]);
            ss = fmaf(f[2 * j], f[2 * j], ss);
            ss = fmaf(f[2 * j + 1], f[2 * j + 1], ss);
        }
        ss = wave_sum(ss);
        float inv = rsqrtf(ss * (1.0f / D) + 1e-6f);
        u32x4 o;
#pragma unroll
        for (int j = 0; j < 4; ++j) o[j] = pack_bf16(f[2 * j] * inv * gf[2 * j], f[2 * j + 1] * inv * gf[2 * j + 1]);
        out[(size_t)(row0 + r) * CH + lane] = o;
    }
}

extern "C" void kernel_launch(void* const* d_in, const int* in_sizes, int n_in,
                              void* d_out, int out_size, void* d_ws, size_t ws_size,
                              hipStream_t stream) {
    const int nrows = in_sizes[0] / D;
    const int rows_per_block = ROWS * (TPB / 64);
    rmsnorm_kernel<<<nrows / rows_per_block, TPB, 0, stream>>>((const u32x4*)d_in[0], (const u32x4*)d_in[1],
                                                              (u32x4*)d_out, nrows);
}
```

```cpp
#include <hip/hip_runtime.h>
#include <stdint.h>
#define STR2(x) #x
#define STR(x) STR2(x)

#define UNPK(dlo, dhi, src) \
    "v_lshlrev_b32 v" #dlo ", 16, v" #src "\n\t" \
    "v_and_b32 v" #dhi ", 0xffff0000, v" #src "\n\t"

#define DEF_ASM16(NAME, LDF, STF, DELAY) \
__global__ __launch_bounds__(256) void NAME(const void* __restrict__ x, const void* __restrict__ g, void* __restrict__ out) { \
    __shared__ uint4 gs[256]; \
    asm volatile( \
    "s_load_dwordx2 s[4:5], %0, 0x0\n\t" \
    "s_load_dwordx2 s[8:9], %0, 0x8\n\t" \
    "s_load_dwordx2 s[12:13], %0, 0x10\n\t" \
    "s_memrealtime s[16:17]\n\t" \
    "s_mov_b32 s6, 0xe00000\n\t" \
    "s_mov_b32 s7, 0x20000\n\t" \
    "s_movk_i32 s10, 0x380\n\t" \
    "s_mov_b32 s11, 0x20000\n\t" \
    "s_mov_b32 s14, 0xe00000\n\t" \
    "s_mov_b32 s15, 0x20000\n\t" \
    "s_mul_i32 s18, %1, 14336\n\t" \
    "v_lshrrev_b32 v1, 4, %2\n\t"                \
    "v_and_b32 v2, 15, %2\n\t"                   \
    "v_lshlrev_b32 v3, 4, v2\n\t"                \
    "v_mad_u32_u24 v4, v1, s10, v3\n\t"          \
    "v_add_u32 v4, s18, v4\n\t"                  \
    "v_bfrev_b32 v5, 1\n\t"                      \
    "v_cmp_gt_u32 vcc, 8, v2\n\t" \
    "v_lshlrev_b32 v7, 4, %2\n\t"                \
    "v_and_b32 v6, 0x3f0, v7\n\t"                \
    "v_cndmask_b32 v5, v5, v4, vcc\n\t"          \
    "v_and_b32 v1, 0xc00, v7\n\t" \
    "v_add_u32 v7, %3, v7\n\t" \
    "v_or_b32 v1, v1, v3\n\t" \
    "v_add_u32 v1, %3, v1\n\t"                   \
    "v_mov_b32 v2, 0\n\t" \
    "v_mov_b32 v3, 0\n\t" \
    "s_waitcnt lgkmcnt(0)\n\t" \
    "buffer_load_dwordx4 v[24:27], v6, s[8:11], 0 offen\n\t" \
    "buffer_load_dwordx4 v[8:11], v4, s[4:7], 0 offen " LDF "\n\t" \
    "buffer_load_dwordx4 v[12:15], v4, s[4:7], 0 offen offset:256 " LDF "\n\t" \
    "buffer_load_dwordx4 v[16:19], v4, s[4:7], 0 offen offset:512 " LDF "\n\t" \
    "buffer_load_dwordx4 v[20:23], v5, s[4:7], 0 offen offset:768 " LDF "\n\t" \
    "v_mov_b32 v72, 0x358637bd\n\t"              \
    "s_waitcnt vmcnt(4)\n\t" \
    "ds_write_b128 v7, v[24:27]\n\t" \
    "ds_read_b128 v[24:27], v1\n\t" \
    "ds_read_b128 v[28:31], v1 offset:256\n\t" \
    "ds_read_b128 v[32:35], v1 offset:512\n\t" \
    "ds_read_b128 v[36:39], v1 offset:768\n\t" \
    "s_waitcnt lgkmcnt(3)\n\t" \
    UNPK(40, 41, 24) UNPK(42, 43, 25) UNPK(44, 45, 26) UNPK(46, 47, 27) \
    "s_waitcnt lgkmcnt(2)\n\t" \
    UNPK(48, 49, 28) UNPK(50, 51, 29) UNPK(52, 53, 30) UNPK(54, 55, 31) \
    "s_waitcnt lgkmcnt(1)\n\t" \
    UNPK(56, 57, 32) UNPK(58, 59, 33) UNPK(60, 61, 34) UNPK(62, 63, 35) \
    "s_waitcnt lgkmcnt(0)\n\t" \
    UNPK(64, 65, 36) UNPK(66, 67, 37) UNPK(68, 69, 38) UNPK(70, 71, 39) \
      \
    "s_waitcnt vmcnt(3)\n\t" \
    "v_dot2c_f32_bf16 v2, v8, v8\n\t" \
    "v_dot2c_f32_bf16 v3, v9, v9\n\t" \
    "v_dot2c_f32_bf16 v2, v10, v10\n\t" \
    "v_dot2c_f32_bf16 v3, v11, v11\n\t" \
    UNPK(24, 25, 8) UNPK(26, 27, 9) UNPK(28, 29, 10) UNPK(30, 31, 11) \
    "v_pk_mul_f32 v[40:41], v[24:25], v[40:41]\n\t" \
    "v_pk_mul_f32 v[42:43], v[26:27], v[42:43]\n\t" \
    "v_pk_mul_f32 v[44:45], v[28:29], v[44:45]\n\t" \
    "v_pk_mul_f32 v[46:47], v[30:31], v[46:47]\n\t" \
      \
    "s_waitcnt vmcnt(2)\n\t" \
    "v_dot2c_f32_bf16 v2, v12, v12\n\t" \
    "v_dot2c_f32_bf16 v3, v13, v13\n\t" \
    "v_dot2c_f32_bf16 v2, v14, v14\n\t" \
    "v_dot2c_f32_bf16 v3, v15, v15\n\t" \
    UNPK(32, 33, 12) UNPK(34, 35, 13) UNPK(36, 37, 14) UNPK(38, 39, 15) \
    "v_pk_mul_f32 v[48:49], v[32:33], v[48:49]\n\t" \
    "v_pk_mul_f32 v[50:51], v[34:35], v[50:51]\n\t" \
    "v_pk_mul_f32 v[52:53], v[36:37], v[52:53]\n\t" \
    "v_pk_mul_f32 v[54:55], v[38:39], v[54:55]\n\t" \
      \
    "s_waitcnt vmcnt(1)\n\t" \
    "v_dot2c_f32_bf16 v2, v16, v16\n\t" \
    "v_dot2c_f32_bf16 v3, v17, v17\n\t" \
    "v_dot2c_f32_bf16 v2, v18, v18\n\t" \
    "v_dot2c_f32_bf16 v3, v19, v19\n\t" \
    "s_waitcnt vmcnt(0)\n\t" \
    "s_memrealtime s[20:21]\n\t" \
    "v_dot2c_f32_bf16 v2, v20, v20\n\t" \
    "v_dot2c_f32_bf16 v3, v21, v21\n\t" \
    "v_dot2c_f32_bf16 v2, v22, v22\n\t" \
    "v_dot2c_f32_bf16 v3, v23, v23\n\t" \
    UNPK(24, 25, 16) UNPK(26, 27, 17) \
    "v_add_f32 v2, v2, v3\n\t" \
    UNPK(28, 29, 18) \
    "v_add_f32_dpp v2, v2, v2 quad_perm:[1,0,3,2] row_mask:0xf bank_mask:0xf bound_ctrl:1\n\t" \
    UNPK(30, 31, 19) \
    "v_add_f32_dpp v2, v2, v2 quad_perm:[2,3,0,1] row_mask:0xf bank_mask:0xf bound_ctrl:1\n\t" \
    "v_pk_mul_f32 v[56:57], v[24:25], v[56:57]\n\t" \
    "v_pk_mul_f32 v[58:59], v[26:27], v[58:59]\n\t" \
    "v_add_f32_dpp v2, v2, v2 row_half_mirror row_mask:0xf bank_mask:0xf bound_ctrl:1\n\t" \
    "v_pk_mul_f32 v[60:61], v[28:29], v[60:61]\n\t" \
    "v_pk_mul_f32 v[62:63], v[30:31], v[62:63]\n\t" \
    "v_add_f32_dpp v2, v2, v2 row_mirror row_mask:0xf bank_mask:0xf bound_ctrl:1\n\t" \
    UNPK(32, 33, 20) \
    "v_fmac_f32 v72, 0x3b124925, v2\n\t" \
    UNPK(34, 35, 21) \
    "v_rsq_f32 v2, v72\n\t" \
    UNPK(36, 37, 22) UNPK(38, 39, 23) \
    "v_pk_mul_f32 v[64:65], v[32:33], v[64:65]\n\t" \
    "v_pk_mul_f32 v[66:67], v[34:35], v[66:67]\n\t" \
    "v_pk_mul_f32 v[68:69], v[36:37], v[68:69]\n\t" \
    "v_pk_mul_f32 v[70:71], v[38:39], v[70:71]\n\t" \
      \
    "v_pk_mul_f32 v[40:41], v[2:3], v[40:41] op_sel_hi:[0,1]\n\t" \
    "v_pk_mul_f32 v[42:43], v[2:3], v[42:43] op_sel_hi:[0,1]\n\t" \
    "v_pk_mul_f32 v[44:45], v[2:3], v[44:45] op_sel_hi:[0,1]\n\t" \
    "v_pk_mul_f32 v[46:47], v[2:3], v[46:47] op_sel_hi:[0,1]\n\t" \
    "v_pk_mul_f32 v[48:49], v[2:3], v[48:49] op_sel_hi:[0,1]\n\t" \
    "v_pk_mul_f32 v[50:51], v[2:3], v[50:51] op_sel_hi:[0,1]\n\t" \
    "v_pk_mul_f32 v[52:53], v[2:3], v[52:53] op_sel_hi:[0,1]\n\t" \
    "v_pk_mul_f32 v[54:55], v[2:3], v[54:55] op_sel_hi:[0,1]\n\t" \
    "v_cvt_pk_bf16_f32 v8, v40, v41\n\t" \
    "v_cvt_pk_bf16_f32 v9, v42, v43\n\t" \
    "v_cvt_pk_bf16_f32 v10, v44, v45\n\t" \
    "v_cvt_pk_bf16_f32 v11, v46, v47\n\t" \
    "v_pk_mul_f32 v[56:57], v[2:3], v[56:57] op_sel_hi:[0,1]\n\t" \
    "v_pk_mul_f32 v[58:59], v[2:3], v[58:59] op_sel_hi:[0,1]\n\t" \
    "v_pk_mul_f32 v[60:61], v[2:3], v[60:61] op_sel_hi:[0,1]\n\t" \
    "v_pk_mul_f32 v[62:63], v[2:3], v[62:63] op_sel_hi:[0,1]\n\t" \
    "v_cvt_pk_bf16_f32 v12, v48, v49\n\t" \
    "v_cvt_pk_bf16_f32 v13, v50, v51\n\t" \
    "v_cvt_pk_bf16_f32 v14, v52, v53\n\t" \
    "v_cvt_pk_bf16_f32 v15, v54, v55\n\t" \
    "v_pk_mul_f32 v[64:65], v[2:3], v[64:65] op_sel_hi:[0,1]\n\t" \
    "v_pk_mul_f32 v[66:67], v[2:3], v[66:67] op_sel_hi:[0,1]\n\t" \
    "v_pk_mul_f32 v[68:69], v[2:3], v[68:69] op_sel_hi:[0,1]\n\t" \
    "v_pk_mul_f32 v[70:71], v[2:3], v[70:71] op_sel_hi:[0,1]\n\t" \
    "v_cvt_pk_bf16_f32 v16, v56, v57\n\t" \
    "v_cvt_pk_bf16_f32 v17, v58, v59\n\t" \
    "v_cvt_pk_bf16_f32 v18, v60, v61\n\t" \
    "v_cvt_pk_bf16_f32 v19, v62, v63\n\t" \
    "v_cvt_pk_bf16_f32 v20, v64, v65\n\t" \
    "v_cvt_pk_bf16_f32 v21, v66, v67\n\t" \
    "v_cvt_pk_bf16_f32 v22, v68, v69\n\t" \
    "v_cvt_pk_bf16_f32 v23, v70, v71\n\t" \
      \
    "1:\n\t" \
    "s_waitcnt lgkmcnt(0)\n\t" \
    "s_sub_u32 s20, s20, s16\n\t" \
    "s_cmp_ge_u32 s20, " STR(DELAY) "\n\t" \
    "s_cbranch_scc1 2f\n\t" \
    "s_sleep 1\n\t" \
    "s_memrealtime s[20:21]\n\t" \
    "s_branch 1b\n\t" \
    "2:\n\t" \
    "buffer_store_dwordx4 v[8:11], v4, s[12:15], 0 offen " STF "\n\t" \
    "buffer_store_dwordx4 v[12:15], v4, s[12:15], 0 offen offset:256 " STF "\n\t" \
    "buffer_store_dwordx4 v[16:19], v4, s[12:15], 0 offen offset:512 " STF "\n\t" \
    "buffer_store_dwordx4 v[20:23], v5, s[12:15], 0 offen offset:768 " STF "\n\t" \
    :: "s"(__builtin_amdgcn_kernarg_segment_ptr()), "s"(__builtin_amdgcn_workgroup_id_x()), "v"(__builtin_amdgcn_workitem_id_x()), "s"((unsigned)(uintptr_t)gs) \
    : "memory", "vcc", "s4", "s5", "s6", "s7", "s8", "s9", "s10", "s11", "s12", "s13", "s14", "s15", "s16", "s17", "s18", "s20", "s21", \
      "v1", "v2", "v3", "v4", "v5", "v6", "v7", "v8", "v9", "v10", "v11", "v12", "v13", "v14", "v15", "v16", "v17", "v18", "v19", "v20", "v21", "v22", "v23", \
      "v24", "v25", "v26", "v27", "v28", "v29", "v30", "v31", "v32", "v33", "v34", "v35", "v36", "v37", "v38", "v39", "v40", "v41", "v42", "v43", "v44", "v45", "v46", "v47", \
      "v48", "v49", "v50", "v51", "v52", "v53", "v54", "v55", "v56", "v57", "v58", "v59", "v60", "v61", "v62", "v63", "v64", "v65", "v66", "v67", "v68", "v69", "v70", "v71", "v72"); \
}

DEF_ASM16(rmsnorm_kernel, "", "nt", 210)
extern "C" void kernel_launch(void* const* d_in, const int* in_sizes, int n_in,
                              void* d_out, int out_size, void* d_ws, size_t ws_size,
                              hipStream_t stream) {
    rmsnorm_kernel<<<1024, 256, 0, stream>>>(d_in[0], d_in[1], d_out);
}
```

```cpp
#include <hip/hip_runtime.h>
#include <hip/hip_bf16.h>
#include <stdint.h>

#define RN_ROWS 16384
#define RN_D 448

#define UNPK(dlo, dhi, src)                         \
    "v_lshlrev_b32 v" #dlo ", 16, v" #src "\n\t"    \
    "v_and_b32 v" #dhi ", s22, v" #src "\n\t"
#define MULG(d, a) "v_pk_mul_f32 v[" #d "], v[" #a "], v[" #d "]\n\t"
#define MULI(d) "v_pk_mul_f32 v[" #d "], v[2:3], v[" #d "] op_sel_hi:[0,1]\n\t"
#define PACK(d, a, b) "v_cvt_pk_bf16_f32 v" #d ", v" #a ", v" #b "\n\t"
#define DOT(acc, r) "v_dot2c_f32_bf16 v" #acc ", v" #r ", v" #r "\n\t"
#define DPPADD(ctrl) "v_add_f32_dpp v2, v2, v2 " ctrl " row_mask:0xf bank_mask:0xf bound_ctrl:1\n\t"

__global__ __launch_bounds__(256) void rmsnorm448_kernel(const void* __restrict__ x, const void* __restrict__ g,
                                                         void* __restrict__ out) {
    __shared__ uint4 gs[256];
    asm volatile(
        "s_load_dwordx2 s[4:5], %0, 0x0\n\t"
        "s_load_dwordx2 s[8:9], %0, 0x8\n\t"
        "s_load_dwordx2 s[12:13], %0, 0x10\n\t"
        "s_mov_b32 s22, 0xffff0000\n\t"
        "s_mov_b32 s6, 0xe00000\n\t"
        "s_mov_b32 s7, 0x20000\n\t"
        "s_movk_i32 s10, 0x380\n\t"
        "s_mov_b32 s11, s7\n\t"
        "s_mov_b32 s14, s6\n\t"
        "s_mov_b32 s15, s7\n\t"
        "s_mul_i32 s18, %1, 14336\n\t"
        "v_lshrrev_b32 v1, 4, %2\n\t"
        "v_and_b32 v2, 15, %2\n\t"
        "v_lshlrev_b32 v3, 4, v2\n\t"
        "v_mad_u32_u24 v4, v1, s10, v3\n\t"
        "v_add_u32 v4, s18, v4\n\t"
        "v_bfrev_b32 v5, 1\n\t"
        "v_cmp_gt_u32 vcc, 8, v2\n\t"
        "v_lshlrev_b32 v7, 4, %2\n\t"
        "v_and_b32 v6, 0x3f0, v7\n\t"
        "v_cndmask_b32 v5, v5, v4, vcc\n\t"
        "v_and_b32 v1, 0xc00, v7\n\t"
        "v_add_u32 v7, %3, v7\n\t"
        "v_or_b32 v1, v1, v3\n\t"
        "v_add_u32 v1, %3, v1\n\t"
        "v_mov_b32 v2, 0\n\t"
        "v_mov_b32 v3, 0\n\t"
        "v_mov_b32 v72, 0x358637bd\n\t"
        "s_waitcnt lgkmcnt(0)\n\t"
        "buffer_load_dwordx4 v[24:27], v6, s[8:11], 0 offen\n\t"
        "buffer_load_dwordx4 v[8:11], v4, s[4:7], 0 offen\n\t"
        "buffer_load_dwordx4 v[12:15], v4, s[4:7], 0 offen offset:256\n\t"
        "buffer_load_dwordx4 v[16:19], v4, s[4:7], 0 offen offset:512\n\t"
        "buffer_load_dwordx4 v[20:23], v5, s[4:7], 0 offen offset:768\n\t"
        "s_memrealtime s[16:17]\n\t"
        "s_waitcnt vmcnt(4)\n\t"
        "ds_write_b128 v7, v[24:27]\n\t"
        "ds_read_b128 v[24:27], v1\n\t"
        "ds_read_b128 v[28:31], v1 offset:256\n\t"
        "ds_read_b128 v[32:35], v1 offset:512\n\t"
        "ds_read_b128 v[36:39], v1 offset:768\n\t"
        "s_waitcnt lgkmcnt(0)\n\t"
        UNPK(40, 41, 24) UNPK(42, 43, 25) UNPK(44, 45, 26) UNPK(46, 47, 27)
        UNPK(48, 49, 28) UNPK(50, 51, 29) UNPK(52, 53, 30) UNPK(54, 55, 31)
        UNPK(56, 57, 32) UNPK(58, 59, 33) UNPK(60, 61, 34) UNPK(62, 63, 35)
        UNPK(64, 65, 36) UNPK(66, 67, 37) UNPK(68, 69, 38) UNPK(70, 71, 39)
        "s_waitcnt vmcnt(3)\n\t"
        DOT(2, 8) DOT(3, 9) DOT(2, 10) DOT(3, 11)
        UNPK(24, 25, 8) UNPK(26, 27, 9) UNPK(28, 29, 10) UNPK(30, 31, 11)
        MULG(40:41, 24:25) MULG(42:43, 26:27) MULG(44:45, 28:29) MULG(46:47, 30:31)
        "s_waitcnt vmcnt(2)\n\t"
        DOT(2, 12) DOT(3, 13) DOT(2, 14) DOT(3, 15)
        UNPK(32, 33, 12) UNPK(34, 35, 13) UNPK(36, 37, 14) UNPK(38, 39, 15)
        MULG(48:49, 32:33) MULG(50:51, 34:35) MULG(52:53, 36:37) MULG(54:55, 38:39)
        "s_waitcnt vmcnt(1)\n\t"
        DOT(2, 16) DOT(3, 17) DOT(2, 18) DOT(3, 19)
        "s_waitcnt vmcnt(0)\n\t"
        "s_memrealtime s[20:21]\n\t"
        DOT(2, 20) DOT(3, 21) DOT(2, 22) DOT(3, 23)
        UNPK(24, 25, 16) UNPK(26, 27, 17)
        "v_add_f32 v2, v2, v3\n\t"
        UNPK(28, 29, 18)
        DPPADD("quad_perm:[1,0,3,2]")
        UNPK(30, 31, 19)
        DPPADD("quad_perm:[2,3,0,1]")
        MULG(56:57, 24:25) MULG(58:59, 26:27)
        DPPADD("row_half_mirror")
        MULG(60:61, 28:29) MULG(62:63, 30:31)
        DPPADD("row_mirror")
        UNPK(32, 33, 20)
        "v_fmac_f32 v72, 0x3b124925, v2\n\t"
        UNPK(34, 35, 21)
        "v_rsq_f32 v2, v72\n\t"
        UNPK(36, 37, 22) UNPK(38, 39, 23)
        MULG(64:65, 32:33) MULG(66:67, 34:35) MULG(68:69, 36:37) MULG(70:71, 38:39)
        MULI(40:41) MULI(42:43) MULI(44:45) MULI(46:47)
        MULI(48:49) MULI(50:51) MULI(52:53) MULI(54:55)
        PACK(8, 40, 41) PACK(9, 42, 43) PACK(10, 44, 45) PACK(11, 46, 47)
        MULI(56:57) MULI(58:59) MULI(60:61) MULI(62:63)
        PACK(12, 48, 49) PACK(13, 50, 51) PACK(14, 52, 53) PACK(15, 54, 55)
        MULI(64:65) MULI(66:67) MULI(68:69) MULI(70:71)
        PACK(16, 56, 57) PACK(17, 58, 59) PACK(18, 60, 61) PACK(19, 62, 63)
        PACK(20, 64, 65) PACK(21, 66, 67) PACK(22, 68, 69) PACK(23, 70, 71)
        "1:\n\t"
        "s_waitcnt lgkmcnt(0)\n\t"
        "s_sub_u32 s20, s20, s16\n\t"
        "s_cmp_ge_u32 s20, 210\n\t"
        "s_cbranch_scc1 2f\n\t"
        "s_sleep 1\n\t"
        "s_memrealtime s[20:21]\n\t"
        "s_branch 1b\n\t"
        "2:\n\t"
        "buffer_store_dwordx4 v[8:11], v4, s[12:15], 0 offen nt\n\t"
        "buffer_store_dwordx4 v[12:15], v4, s[12:15], 0 offen offset:256 nt\n\t"
        "buffer_store_dwordx4 v[16:19], v4, s[12:15], 0 offen offset:512 nt\n\t"
        "buffer_store_dwordx4 v[20:23], v5, s[12:15], 0 offen offset:768 nt\n\t"
        :
        : "s"(__builtin_amdgcn_kernarg_segment_ptr()), "s"(__builtin_amdgcn_workgroup_id_x()),
          "v"(__builtin_amdgcn_workitem_id_x()), "s"((unsigned)(uintptr_t)gs)
        : "memory", "vcc", "scc", "s4", "s5", "s6", "s7", "s8", "s9", "s10", "s11", "s12", "s13", "s14", "s15", "s16",
          "s17", "s18", "s20", "s21", "s22", "v1", "v2", "v3", "v4", "v5", "v6", "v7", "v8", "v9", "v10", "v11",
          "v12", "v13", "v14", "v15", "v16", "v17", "v18", "v19", "v20", "v21", "v22", "v23", "v24", "v25", "v26",
          "v27", "v28", "v29", "v30", "v31", "v32", "v33", "v34", "v35", "v36", "v37", "v38", "v39", "v40", "v41",
          "v42", "v43", "v44", "v45", "v46", "v47", "v48", "v49", "v50", "v51", "v52", "v53", "v54", "v55", "v56",
          "v57", "v58", "v59", "v60", "v61", "v62", "v63", "v64", "v65", "v66", "v67", "v68", "v69", "v70", "v71",
          "v72");
}

__global__ __launch_bounds__(256) void rmsnorm_generic_kernel(const __hip_bfloat16* __restrict__ x,
                                                              const __hip_bfloat16* __restrict__ g,
                                                              __hip_bfloat16* __restrict__ out, int rows, int d) {
    const int row = blockIdx.x * 4 + (threadIdx.x >> 6);
    const int lane = threadIdx.x & 63;
    if (row >= rows) return;
    const __hip_bfloat16* xr = x + (size_t)row * d;
    float ss = 0.f;
    for (int c = lane; c < d; c += 64) {
        const float v = __bfloat162float(xr[c]);
        ss = fmaf(v, v, ss);
    }
    for (int o = 32; o > 0; o >>= 1) ss += __shfl_xor(ss, o, 64);
    const float inv = rsqrtf(ss / (float)d + 1e-6f);
    for (int c = lane; c < d; c += 64)
        out[(size_t)row * d + c] = __float2bfloat16(__bfloat162float(xr[c]) * inv * __bfloat162float(g[c]));
}

extern "C" void kernel_launch(void* const* d_in, const int* in_sizes, int n_in, void* d_out, int out_size,
                              void* d_ws, size_t ws_size, hipStream_t stream) {
    const int d = in_sizes[1];
    const int rows = in_sizes[0] / d;
    if (d == RN_D && rows == RN_ROWS) {
        rmsnorm448_kernel<<<RN_ROWS / 16, 256, 0, stream>>>(d_in[0], d_in[1], d_out);
    } else {
        rmsnorm_generic_kernel<<<(rows + 3) / 4, 256, 0, stream>>>((const __hip_bfloat16*)d_in[0],
                                                                  (const __hip_bfloat16*)d_in[1],
                                                                  (__hip_bfloat16*)d_out, rows, d);
    }
}
```

```cpp
#include <hip/hip_runtime.h>
#include <hip/hip_bf16.h>
#include <stdint.h>

#define RN_ROWS 16384
#define RN_D 448

#define UNPK(dlo, dhi, src)                         \
    "v_lshlrev_b32 v" #dlo ", 16, v" #src "\n\t"    \
    "v_and_b32 v" #dhi ", s22, v" #src "\n\t"
#define MULG(d, a) "v_pk_mul_f32 v[" #d "], v[" #a "], v[" #d "]\n\t"
#define MULI(d) "v_pk_mul_f32 v[" #d "], v[2:3], v[" #d "] op_sel_hi:[0,1]\n\t"
#define PACK(d, a, b) "v_cvt_pk_bf16_f32 v" #d ", v" #a ", v" #b "\n\t"
#define DOT(acc, r) "v_dot2c_f32_bf16 v" #acc ", v" #r ", v" #r "\n\t"
#define DPPADD(ctrl) "v_add_f32_dpp v2, v2, v2 " ctrl " row_mask:0xf bank_mask:0xf bound_ctrl:1\n\t"

__global__ __launch_bounds__(256) void rmsnorm448_kernel(const void* __restrict__ x, const void* __restrict__ g,
                                                         void* __restrict__ out) {
    __shared__ uint4 gs[256];
    asm volatile(
        "s_load_dwordx2 s[4:5], %0, 0x0\n\t"
        "s_load_dwordx2 s[8:9], %0, 0x8\n\t"
        "s_load_dwordx2 s[12:13], %0, 0x10\n\t"
        "s_mov_b32 s22, 0xffff0000\n\t"
        "s_mov_b32 s6, 0xe00000\n\t"
        "s_mov_b32 s7, 0x20000\n\t"
        "s_movk_i32 s10, 0x380\n\t"
        "s_mov_b32 s11, s7\n\t"
        "s_mov_b32 s14, s6\n\t"
        "s_mov_b32 s15, s7\n\t"
        "s_mul_i32 s18, %1, 14336\n\t"
        "v_lshrrev_b32 v1, 4, %2\n\t"
        "v_and_b32 v2, 15, %2\n\t"
        "v_lshlrev_b32 v3, 4, v2\n\t"
        "v_mad_u32_u24 v4, v1, s10, v3\n\t"
        "v_add_u32 v4, s18, v4\n\t"
        "v_bfrev_b32 v5, 1\n\t"
        "v_cmp_gt_u32 vcc, 8, v2\n\t"
        "v_lshlrev_b32 v7, 4, %2\n\t"
        "v_and_b32 v6, 0x3f0, v7\n\t"
        "v_cndmask_b32 v5, v5, v4, vcc\n\t"
        "v_and_b32 v1, 0xc00, v7\n\t"
        "v_add_u32 v7, %3, v7\n\t"
        "v_or_b32 v1, v1, v3\n\t"
        "v_add_u32 v1, %3, v1\n\t"
        "v_mov_b32 v2, 0\n\t"
        "v_mov_b32 v3, 0\n\t"
        "v_mov_b32 v72, 0x358637bd\n\t"
        "s_waitcnt lgkmcnt(0)\n\t"
        "buffer_load_dwordx4 v[24:27], v6, s[8:11], 0 offen\n\t"
        "buffer_load_dwordx4 v[8:11], v4, s[4:7], 0 offen nt\n\t"
        "buffer_load_dwordx4 v[12:15], v4, s[4:7], 0 offen offset:256 nt\n\t"
        "buffer_load_dwordx4 v[16:19], v4, s[4:7], 0 offen offset:512 nt\n\t"
        "buffer_load_dwordx4 v[20:23], v5, s[4:7], 0 offen offset:768 nt\n\t"
        "s_memrealtime s[16:17]\n\t"
        "s_waitcnt vmcnt(4)\n\t"
        "ds_write_b128 v7, v[24:27]\n\t"
        "ds_read_b128 v[24:27], v1\n\t"
        "ds_read_b128 v[28:31], v1 offset:256\n\t"
        "ds_read_b128 v[32:35], v1 offset:512\n\t"
        "ds_read_b128 v[36:39], v1 offset:768\n\t"
        "s_waitcnt lgkmcnt(0)\n\t"
        UNPK(40, 41, 24) UNPK(42, 43, 25) UNPK(44, 45, 26) UNPK(46, 47, 27)
        UNPK(48, 49, 28) UNPK(50, 51, 29) UNPK(52, 53, 30) UNPK(54, 55, 31)
        UNPK(56, 57, 32) UNPK(58, 59, 33) UNPK(60, 61, 34) UNPK(62, 63, 35)
        UNPK(64, 65, 36) UNPK(66, 67, 37) UNPK(68, 69, 38) UNPK(70, 71, 39)
        "s_waitcnt vmcnt(3)\n\t"
        DOT(2, 8) DOT(3, 9) DOT(2, 10) DOT(3, 11)
        UNPK(24, 25, 8) UNPK(26, 27, 9) UNPK(28, 29, 10) UNPK(30, 31, 11)
        MULG(40:41, 24:25) MULG(42:43, 26:27) MULG(44:45, 28:29) MULG(46:47, 30:31)
        "s_waitcnt vmcnt(2)\n\t"
        DOT(2, 12) DOT(3, 13) DOT(2, 14) DOT(3, 15)
        UNPK(32, 33, 12) UNPK(34, 35, 13) UNPK(36, 37, 14) UNPK(38, 39, 15)
        MULG(48:49, 32:33) MULG(50:51, 34:35) MULG(52:53, 36:37) MULG(54:55, 38:39)
        "s_waitcnt vmcnt(1)\n\t"
        DOT(2, 16) DOT(3, 17) DOT(2, 18) DOT(3, 19)
        "s_waitcnt vmcnt(0)\n\t"
        "s_memrealtime s[20:21]\n\t"
        DOT(2, 20) DOT(3, 21) DOT(2, 22) DOT(3, 23)
        UNPK(24, 25, 16) UNPK(26, 27, 17)
        "v_add_f32 v2, v2, v3\n\t"
        UNPK(28, 29, 18)
        DPPADD("quad_perm:[1,0,3,2]")
        UNPK(30, 31, 19)
        DPPADD("quad_perm:[2,3,0,1]")
        MULG(56:57, 24:25) MULG(58:59, 26:27)
        DPPADD("row_half_mirror")
        MULG(60:61, 28:29) MULG(62:63, 30:31)
        DPPADD("row_mirror")
        UNPK(32, 33, 20)
        "v_fmac_f32 v72, 0x3b124925, v2\n\t"
        UNPK(34, 35, 21)
        "v_rsq_f32 v2, v72\n\t"
        UNPK(36, 37, 22) UNPK(38, 39, 23)
        MULG(64:65, 32:33) MULG(66:67, 34:35) MULG(68:69, 36:37) MULG(70:71, 38:39)
        MULI(40:41) MULI(42:43) MULI(44:45) MULI(46:47)
        MULI(48:49) MULI(50:51) MULI(52:53) MULI(54:55)
        PACK(8, 40, 41) PACK(9, 42, 43) PACK(10, 44, 45) PACK(11, 46, 47)
        MULI(56:57) MULI(58:59) MULI(60:61) MULI(62:63)
        PACK(12, 48, 49) PACK(13, 50, 51) PACK(14, 52, 53) PACK(15, 54, 55)
        MULI(64:65) MULI(66:67) MULI(68:69) MULI(70:71)
        PACK(16, 56, 57) PACK(17, 58, 59) PACK(18, 60, 61) PACK(19, 62, 63)
        PACK(20, 64, 65) PACK(21, 66, 67) PACK(22, 68, 69) PACK(23, 70, 71)
        "1:\n\t"
        "s_waitcnt lgkmcnt(0)\n\t"
        "s_sub_u32 s20, s20, s16\n\t"
        "s_cmp_ge_u32 s20, 210\n\t"
        "s_cbranch_scc1 2f\n\t"
        "s_sleep 1\n\t"
        "s_memrealtime s[20:21]\n\t"
        "s_branch 1b\n\t"
        "2:\n\t"
        "buffer_store_dwordx4 v[8:11], v4, s[12:15], 0 offen nt\n\t"
        "buffer_store_dwordx4 v[12:15], v4, s[12:15], 0 offen offset:256 nt\n\t"
        "buffer_store_dwordx4 v[16:19], v4, s[12:15], 0 offen offset:512 nt\n\t"
        "buffer_store_dwordx4 v[20:23], v5, s[12:15], 0 offen offset:768 nt\n\t"
        :
        : "s"(__builtin_amdgcn_kernarg_segment_ptr()), "s"(__builtin_amdgcn_workgroup_id_x()),
          "v"(__builtin_amdgcn_workitem_id_x()), "s"((unsigned)(uintptr_t)gs)
        : "memory", "vcc", "scc", "s4", "s5", "s6", "s7", "s8", "s9", "s10", "s11", "s12", "s13", "s14", "s15", "s16",
          "s17", "s18", "s20", "s21", "s22", "v1", "v2", "v3", "v4", "v5", "v6", "v7", "v8", "v9", "v10", "v11",
          "v12", "v13", "v14", "v15", "v16", "v17", "v18", "v19", "v20", "v21", "v22", "v23", "v24", "v25", "v26",
          "v27", "v28", "v29", "v30", "v31", "v32", "v33", "v34", "v35", "v36", "v37", "v38", "v39", "v40", "v41",
          "v42", "v43", "v44", "v45", "v46", "v47", "v48", "v49", "v50", "v51", "v52", "v53", "v54", "v55", "v56",
          "v57", "v58", "v59", "v60", "v61", "v62", "v63", "v64", "v65", "v66", "v67", "v68", "v69", "v70", "v71",
          "v72");
}

__global__ __launch_bounds__(256) void rmsnorm_generic_kernel(const __hip_bfloat16* __restrict__ x,
                                                              const __hip_bfloat16* __restrict__ g,
                                                              __hip_bfloat16* __restrict__ out, int rows, int d) {
    const int row = blockIdx.x * 4 + (threadIdx.x >> 6);
    const int lane = threadIdx.x & 63;
    if (row >= rows) return;
    const __hip_bfloat16* xr = x + (size_t)row * d;
    float ss = 0.f;
    for (int c = lane; c < d; c += 64) {
        const float v = __bfloat162float(xr[c]);
        ss = fmaf(v, v, ss);
    }
    for (int o = 32; o > 0; o >>= 1) ss += __shfl_xor(ss, o, 64);
    const float inv = rsqrtf(ss / (float)d + 1e-6f);
    for (int c = lane; c < d; c += 64)
        out[(size_t)row * d + c] = __float2bfloat16(__bfloat162float(xr[c]) * inv * __bfloat162float(g[c]));
}

extern "C" void kernel_launch(void* const* d_in, const int* in_sizes, int n_in, void* d_out, int out_size,
                              void* d_ws, size_t ws_size, hipStream_t stream) {
    const int d = in_sizes[1];
    const int rows = in_sizes[0] / d;
    if (d == RN_D && rows == RN_ROWS) {
        rmsnorm448_kernel<<<RN_ROWS / 16, 256, 0, stream>>>(d_in[0], d_in[1], d_out);
    } else {
        rmsnorm_generic_kernel<<<(rows + 3) / 4, 256, 0, stream>>>((const __hip_bfloat16*)d_in[0],
                                                                  (const __hip_bfloat16*)d_in[1],
                                                                  (__hip_bfloat16*)d_out, rows, d);
    }
}
```

```cpp
#include <hip/hip_runtime.h>
#include <hip/hip_bf16.h>
#include <stdint.h>

#define RN_ROWS 16384
#define RN_D 448

#define UNPK(dlo, dhi, src)                         \
    "v_lshlrev_b32 v" #dlo ", 16, v" #src "\n\t"    \
    "v_and_b32 v" #dhi ", s22, v" #src "\n\t"
#define MULG(d, a) "v_pk_mul_f32 v[" #d "], v[" #a "], v[" #d "]\n\t"
#define MULI(d) "v_pk_mul_f32 v[" #d "], v[2:3], v[" #d "] op_sel_hi:[0,1]\n\t"
#define PACK(d, a, b) "v_cvt_pk_bf16_f32 v" #d ", v" #a ", v" #b "\n\t"
#define DOT(acc, r) "v_dot2c_f32_bf16 v" #acc ", v" #r ", v" #r "\n\t"
#define DPPADD(ctrl) "v_add_f32_dpp v2, v2, v2 " ctrl " row_mask:0xf bank_mask:0xf bound_ctrl:1\n\t"

__global__ __launch_bounds__(256) void rmsnorm448_kernel(const void* __restrict__ x, const void* __restrict__ g,
                                                         void* __restrict__ out) {
    __shared__ uint4 gs[256];
    asm volatile(
        "s_load_dwordx2 s[4:5], %0, 0x0\n\t"
        "s_load_dwordx2 s[8:9], %0, 0x8\n\t"
        "s_load_dwordx2 s[12:13], %0, 0x10\n\t"
        "s_mov_b32 s22, 0xffff0000\n\t"
        "s_mov_b32 s6, 0xe00000\n\t"
        "s_mov_b32 s7, 0x20000\n\t"
        "s_movk_i32 s10, 0x380\n\t"
        "s_mov_b32 s11, s7\n\t"
        "s_mov_b32 s14, s6\n\t"
        "s_mov_b32 s15, s7\n\t"
        "s_mul_i32 s18, %1, 14336\n\t"
        "v_lshrrev_b32 v1, 4, %2\n\t"
        "v_and_b32 v2, 15, %2\n\t"
        "v_lshlrev_b32 v3, 4, v2\n\t"
        "v_mad_u32_u24 v4, v1, s10, v3\n\t"
        "v_add_u32 v4, s18, v4\n\t"
        "v_bfrev_b32 v5, 1\n\t"
        "v_cmp_gt_u32 vcc, 8, v2\n\t"
        "v_lshlrev_b32 v7, 4, %2\n\t"
        "v_and_b32 v6, 0x3f0, v7\n\t"
        "v_cndmask_b32 v5, v5, v4, vcc\n\t"
        "v_and_b32 v1, 0xc00, v7\n\t"
        "v_add_u32 v7, %3, v7\n\t"
        "v_or_b32 v1, v1, v3\n\t"
        "v_add_u32 v1, %3, v1\n\t"
        "v_mov_b32 v2, 0\n\t"
        "v_mov_b32 v3, 0\n\t"
        "v_mov_b32 v72, 0x358637bd\n\t"
        "s_waitcnt lgkmcnt(0)\n\t"
        "buffer_load_dwordx4 v[24:27], v6, s[8:11], 0 offen\n\t"
        "buffer_load_dwordx4 v[8:11], v4, s[4:7], 0 offen nt\n\t"
        "buffer_load_dwordx4 v[12:15], v4, s[4:7], 0 offen offset:256 nt\n\t"
        "buffer_load_dwordx4 v[16:19], v4, s[4:7], 0 offen offset:512 nt\n\t"
        "buffer_load_dwordx4 v[20:23], v5, s[4:7], 0 offen offset:768 nt\n\t"
        "s_memrealtime s[16:17]\n\t"
        "s_waitcnt vmcnt(4)\n\t"
        "ds_write_b128 v7, v[24:27]\n\t"
        "ds_read_b128 v[24:27], v1\n\t"
        "ds_read_b128 v[28:31], v1 offset:256\n\t"
        "ds_read_b128 v[32:35], v1 offset:512\n\t"
        "ds_read_b128 v[36:39], v1 offset:768\n\t"
        "s_waitcnt lgkmcnt(0)\n\t"
        UNPK(40, 41, 24) UNPK(42, 43, 25) UNPK(44, 45, 26) UNPK(46, 47, 27)
        UNPK(48, 49, 28) UNPK(50, 51, 29) UNPK(52, 53, 30) UNPK(54, 55, 31)
        UNPK(56, 57, 32) UNPK(58, 59, 33) UNPK(60, 61, 34) UNPK(62, 63, 35)
        UNPK(64, 65, 36) UNPK(66, 67, 37) UNPK(68, 69, 38) UNPK(70, 71, 39)
        "s_waitcnt vmcnt(3)\n\t"
        DOT(2, 8) DOT(3, 9) DOT(2, 10) DOT(3, 11)
        UNPK(24, 25, 8) UNPK(26, 27, 9) UNPK(28, 29, 10) UNPK(30, 31, 11)
        MULG(40:41, 24:25) MULG(42:43, 26:27) MULG(44:45, 28:29) MULG(46:47, 30:31)
        "s_waitcnt vmcnt(2)\n\t"
        DOT(2, 12) DOT(3, 13) DOT(2, 14) DOT(3, 15)
        UNPK(32, 33, 12) UNPK(34, 35, 13) UNPK(36, 37, 14) UNPK(38, 39, 15)
        MULG(48:49, 32:33) MULG(50:51, 34:35) MULG(52:53, 36:37) MULG(54:55, 38:39)
        "s_waitcnt vmcnt(1)\n\t"
        DOT(2, 16) DOT(3, 17) DOT(2, 18) DOT(3, 19)
        "s_waitcnt vmcnt(0)\n\t"
        "s_memrealtime s[20:21]\n\t"
        DOT(2, 20) DOT(3, 21) DOT(2, 22) DOT(3, 23)
        UNPK(24, 25, 16) UNPK(26, 27, 17)
        "v_add_f32 v2, v2, v3\n\t"
        UNPK(28, 29, 18)
        DPPADD("quad_perm:[1,0,3,2]")
        UNPK(30, 31, 19)
        DPPADD("quad_perm:[2,3,0,1]")
        MULG(56:57, 24:25) MULG(58:59, 26:27)
        DPPADD("row_half_mirror")
        MULG(60:61, 28:29) MULG(62:63, 30:31)
        DPPADD("row_mirror")
        UNPK(32, 33, 20)
        "v_fmac_f32 v72, 0x3b124925, v2\n\t"
        UNPK(34, 35, 21)
        "v_rsq_f32 v2, v72\n\t"
        UNPK(36, 37, 22) UNPK(38, 39, 23)
        MULG(64:65, 32:33) MULG(66:67, 34:35) MULG(68:69, 36:37) MULG(70:71, 38:39)
        MULI(40:41) MULI(42:43) MULI(44:45) MULI(46:47)
        MULI(48:49) MULI(50:51) MULI(52:53) MULI(54:55)
        PACK(8, 40, 41) PACK(9, 42, 43) PACK(10, 44, 45) PACK(11, 46, 47)
        MULI(56:57) MULI(58:59) MULI(60:61) MULI(62:63)
        PACK(12, 48, 49) PACK(13, 50, 51) PACK(14, 52, 53) PACK(15, 54, 55)
        MULI(64:65) MULI(66:67) MULI(68:69) MULI(70:71)
        PACK(16, 56, 57) PACK(17, 58, 59) PACK(18, 60, 61) PACK(19, 62, 63)
        PACK(20, 64, 65) PACK(21, 66, 67) PACK(22, 68, 69) PACK(23, 70, 71)
        "1:\n\t"
        "s_waitcnt lgkmcnt(0)\n\t"
        "s_sub_u32 s20, s20, s16\n\t"
        "s_cmp_ge_u32 s20, 210\n\t"
        "s_cbranch_scc1 2f\n\t"
        "s_sleep 1\n\t"
        "s_memrealtime s[20:21]\n\t"
        "s_branch 1b\n\t"
        "2:\n\t"
        "buffer_store_dwordx4 v[8:11], v4, s[12:15], 0 offen sc1 nt\n\t"
        "buffer_store_dwordx4 v[12:15], v4, s[12:15], 0 offen offset:256 sc1 nt\n\t"
        "buffer_store_dwordx4 v[16:19], v4, s[12:15], 0 offen offset:512 sc1 nt\n\t"
        "buffer_store_dwordx4 v[20:23], v5, s[12:15], 0 offen offset:768 sc1 nt\n\t"
        :
        : "s"(__builtin_amdgcn_kernarg_segment_ptr()), "s"(__builtin_amdgcn_workgroup_id_x()),
          "v"(__builtin_amdgcn_workitem_id_x()), "s"((unsigned)(uintptr_t)gs)
        : "memory", "vcc", "scc", "s4", "s5", "s6", "s7", "s8", "s9", "s10", "s11", "s12", "s13", "s14", "s15", "s16",
          "s17", "s18", "s20", "s21", "s22", "v1", "v2", "v3", "v4", "v5", "v6", "v7", "v8", "v9", "v10", "v11",
          "v12", "v13", "v14", "v15", "v16", "v17", "v18", "v19", "v20", "v21", "v22", "v23", "v24", "v25", "v26",
          "v27", "v28", "v29", "v30", "v31", "v32", "v33", "v34", "v35", "v36", "v37", "v38", "v39", "v40", "v41",
          "v42", "v43", "v44", "v45", "v46", "v47", "v48", "v49", "v50", "v51", "v52", "v53", "v54", "v55", "v56",
          "v57", "v58", "v59", "v60", "v61", "v62", "v63", "v64", "v65", "v66", "v67", "v68", "v69", "v70", "v71",
          "v72");
}

__global__ __launch_bounds__(256) void rmsnorm_generic_kernel(const __hip_bfloat16* __restrict__ x,
                                                              const __hip_bfloat16* __restrict__ g,
                                                              __hip_bfloat16* __restrict__ out, int rows, int d) {
    const int row = blockIdx.x * 4 + (threadIdx.x >> 6);
    const int lane = threadIdx.x & 63;
    if (row >= rows) return;
    const __hip_bfloat16* xr = x + (size_t)row * d;
    float ss = 0.f;
    for (int c = lane; c < d; c += 64) {
        const float v = __bfloat162float(xr[c]);
        ss = fmaf(v, v, ss);
    }
    for (int o = 32; o > 0; o >>= 1) ss += __shfl_xor(ss, o, 64);
    const float inv = rsqrtf(ss / (float)d + 1e-6f);
    for (int c = lane; c < d; c += 64)
        out[(size_t)row * d + c] = __float2bfloat16(__bfloat162float(xr[c]) * inv * __bfloat162float(g[c]));
}

extern "C" void kernel_launch(void* const* d_in, const int* in_sizes, int n_in, void* d_out, int out_size,
                              void* d_ws, size_t ws_size, hipStream_t stream) {
    const int d = in_sizes[1];
    const int rows = in_sizes[0] / d;
    if (d == RN_D && rows == RN_ROWS) {
        rmsnorm448_kernel<<<RN_ROWS / 16, 256, 0, stream>>>(d_in[0], d_in[1], d_out);
    } else {
        rmsnorm_generic_kernel<<<(rows + 3) / 4, 256, 0, stream>>>((const __hip_bfloat16*)d_in[0],
                                                                  (const __hip_bfloat16*)d_in[1],
                                                                  (__hip_bfloat16*)d_out, rows, d);
    }
}
```

```cpp
#include <hip/hip_runtime.h>
#include <hip/hip_bf16.h>
#include <stdint.h>

#define RN_ROWS 16384
#define RN_D 448

#define UNPK(dlo, dhi, src)                         \
    "v_lshlrev_b32 v" #dlo ", 16, v" #src "\n\t"    \
    "v_and_b32 v" #dhi ", s22, v" #src "\n\t"
#define MULG(d, a) "v_pk_mul_f32 v[" #d "], v[" #a "], v[" #d "]\n\t"
#define MULI(d) "v_pk_mul_f32 v[" #d "], v[2:3], v[" #d "] op_sel_hi:[0,1]\n\t"
#define PACK(d, a, b) "v_cvt_pk_bf16_f32 v" #d ", v" #a ", v" #b "\n\t"
#define DOT(acc, r) "v_dot2c_f32_bf16 v" #acc ", v" #r ", v" #r "\n\t"
#define DPPADD(ctrl) "v_add_f32_dpp v2, v2, v2 " ctrl " row_mask:0xf bank_mask:0xf bound_ctrl:1\n\t"

__global__ __launch_bounds__(256) void rmsnorm448_kernel(const void* __restrict__ x, const void* __restrict__ g,
                                                         void* __restrict__ out) {
    __shared__ uint4 gs[256];
    asm volatile(
        "s_load_dwordx2 s[4:5], %0, 0x0\n\t"
        "s_load_dwordx2 s[8:9], %0, 0x8\n\t"
        "s_load_dwordx2 s[12:13], %0, 0x10\n\t"
        "s_mov_b32 s22, 0xffff0000\n\t"
        "s_mov_b32 s6, 0xe00000\n\t"
        "s_mov_b32 s7, 0x20000\n\t"
        "s_movk_i32 s10, 0x380\n\t"
        "s_mov_b32 s11, s7\n\t"
        "s_mov_b32 s14, s6\n\t"
        "s_mov_b32 s15, s7\n\t"
        "s_mul_i32 s18, %1, 14336\n\t"
        "s_movk_i32 s19, 0x100\n\t"
        "v_lshrrev_b32 v1, 4, %2\n\t"
        "v_and_b32 v2, 15, %2\n\t"
        "v_lshlrev_b32 v3, 4, v2\n\t"
        "v_mad_u32_u24 v4, v1, s10, v3\n\t"
        "v_add_u32 v4, s18, v4\n\t"
        "v_bfrev_b32 v5, 1\n\t"
        "v_cmp_gt_u32 vcc, 8, v2\n\t"
        "v_lshlrev_b32 v7, 4, %2\n\t"
        "v_and_b32 v6, 0x3f0, v7\n\t"
        "v_cndmask_b32 v5, v5, v4, vcc\n\t"
        "v_and_b32 v1, 0xc00, v7\n\t"
        "v_add_u32 v7, %3, v7\n\t"
        "v_or_b32 v1, v1, v3\n\t"
        "v_add_u32 v1, %3, v1\n\t"
        "v_mov_b32 v2, 0\n\t"
        "v_mov_b32 v3, 0\n\t"
        "v_mov_b32 v72, 0x358637bd\n\t"
        "s_waitcnt lgkmcnt(0)\n\t"
        "s_and_b32 s9, s9, 0xffff\n\t"
        "s_and_b32 s5, s5, 0xffff\n\t"
        "buffer_load_dwordx4 v[24:27], v6, s[8:11], 0 offen\n\t"
        "buffer_load_dwordx4 v[8:11], v4, s[4:7], 0 offen\n\t"
        "buffer_load_dwordx4 v[12:15], v4, s[4:7], 0 offen offset:256\n\t"
        "buffer_load_dwordx4 v[16:19], v4, s[4:7], 0 offen offset:512\n\t"
        "buffer_load_dwordx4 v[20:23], v5, s[4:7], 0 offen offset:768\n\t"
        "s_memrealtime s[16:17]\n\t"
        "s_and_b32 s13, s13, 0xffff\n\t"
        "s_waitcnt vmcnt(4)\n\t"
        "ds_write_b128 v7, v[24:27]\n\t"
        "ds_read_b128 v[24:27], v1\n\t"
        "ds_read_b128 v[28:31], v1 offset:256\n\t"
        "ds_read_b128 v[32:35], v1 offset:512\n\t"
        "ds_read_b128 v[36:39], v1 offset:768\n\t"
        "s_waitcnt lgkmcnt(0)\n\t"
        UNPK(40, 41, 24) UNPK(42, 43, 25) UNPK(44, 45, 26) UNPK(46, 47, 27)
        UNPK(48, 49, 28) UNPK(50, 51, 29) UNPK(52, 53, 30) UNPK(54, 55, 31)
        UNPK(56, 57, 32) UNPK(58, 59, 33) UNPK(60, 61, 34) UNPK(62, 63, 35)
        UNPK(64, 65, 36) UNPK(66, 67, 37) UNPK(68, 69, 38) UNPK(70, 71, 39)
        "s_waitcnt vmcnt(3)\n\t"
        DOT(2, 8) DOT(3, 9) DOT(2, 10) DOT(3, 11)
        UNPK(24, 25, 8) UNPK(26, 27, 9) UNPK(28, 29, 10) UNPK(30, 31, 11)
        MULG(40:41, 24:25) MULG(42:43, 26:27) MULG(44:45, 28:29) MULG(46:47, 30:31)
        "s_waitcnt vmcnt(2)\n\t"
        DOT(2, 12) DOT(3, 13) DOT(2, 14) DOT(3, 15)
        UNPK(32, 33, 12) UNPK(34, 35, 13) UNPK(36, 37, 14) UNPK(38, 39, 15)
        MULG(48:49, 32:33) MULG(50:51, 34:35) MULG(52:53, 36:37) MULG(54:55, 38:39)
        "s_waitcnt vmcnt(1)\n\t"
        DOT(2, 16) DOT(3, 17) DOT(2, 18) DOT(3, 19)
        "s_waitcnt vmcnt(0)\n\t"
        "s_memrealtime s[20:21]\n\t"
        DOT(2, 20) DOT(3, 21) DOT(2, 22) DOT(3, 23)
        UNPK(24, 25, 16) UNPK(26, 27, 17)
        "v_add_f32 v2, v2, v3\n\t"
        UNPK(28, 29, 18)
        DPPADD("quad_perm:[1,0,3,2]")
        UNPK(30, 31, 19)
        DPPADD("quad_perm:[2,3,0,1]")
        MULG(56:57, 24:25) MULG(58:59, 26:27)
        DPPADD("row_half_mirror")
        MULG(60:61, 28:29) MULG(62:63, 30:31)
        DPPADD("row_mirror")
        UNPK(32, 33, 20)
        "v_fmac_f32 v72, 0x3b124925, v2\n\t"
        UNPK(34, 35, 21)
        "v_rsq_f32 v2, v72\n\t"
        UNPK(36, 37, 22) UNPK(38, 39, 23)
        MULG(64:65, 32:33) MULG(66:67, 34:35) MULG(68:69, 36:37) MULG(70:71, 38:39)
        MULI(40:41) MULI(42:43) MULI(44:45) MULI(46:47)
        MULI(48:49) MULI(50:51) MULI(52:53) MULI(54:55)
        PACK(8, 40, 41) PACK(9, 42, 43) PACK(10, 44, 45) PACK(11, 46, 47)
        MULI(56:57) MULI(58:59) MULI(60:61) MULI(62:63)
        PACK(12, 48, 49) PACK(13, 50, 51) PACK(14, 52, 53) PACK(15, 54, 55)
        MULI(64:65) MULI(66:67) MULI(68:69) MULI(70:71)
        PACK(16, 56, 57) PACK(17, 58, 59) PACK(18, 60, 61) PACK(19, 62, 63)
        PACK(20, 64, 65) PACK(21, 66, 67) PACK(22, 68, 69) PACK(23, 70, 71)
        "1:\n\t"
        "s_waitcnt lgkmcnt(0)\n\t"
        "s_sub_u32 s20, s20, s16\n\t"
        "s_cmp_ge_u32 s20, 210\n\t"
        "s_cbranch_scc1 2f\n\t"
        "s_sleep 1\n\t"
        "s_memrealtime s[20:21]\n\t"
        "s_sub_u32 s19, s19, 1\n\t"
        "s_cbranch_scc0 1b\n\t"
        "2:\n\t"
        "buffer_store_dwordx4 v[8:11], v4, s[12:15], 0 offen nt\n\t"
        "buffer_store_dwordx4 v[12:15], v4, s[12:15], 0 offen offset:256 nt\n\t"
        "buffer_store_dwordx4 v[16:19], v4, s[12:15], 0 offen offset:512 nt\n\t"
        "buffer_store_dwordx4 v[20:23], v5, s[12:15], 0 offen offset:768 nt\n\t"
        :
        : "s"(__builtin_amdgcn_kernarg_segment_ptr()), "s"(__builtin_amdgcn_workgroup_id_x()),
          "v"(__builtin_amdgcn_workitem_id_x()), "s"((unsigned)(uintptr_t)gs)
        : "memory", "vcc", "scc", "s4", "s5", "s6", "s7", "s8", "s9", "s10", "s11", "s12", "s13", "s14", "s15", "s16",
          "s17", "s18", "s19", "s20", "s21", "s22", "v1", "v2", "v3", "v4", "v5", "v6", "v7", "v8", "v9", "v10", "v11",
          "v12", "v13", "v14", "v15", "v16", "v17", "v18", "v19", "v20", "v21", "v22", "v23", "v24", "v25", "v26",
          "v27", "v28", "v29", "v30", "v31", "v32", "v33", "v34", "v35", "v36", "v37", "v38", "v39", "v40", "v41",
          "v42", "v43", "v44", "v45", "v46", "v47", "v48", "v49", "v50", "v51", "v52", "v53", "v54", "v55", "v56",
          "v57", "v58", "v59", "v60", "v61", "v62", "v63", "v64", "v65", "v66", "v67", "v68", "v69", "v70", "v71",
          "v72");
}

__global__ __launch_bounds__(256) void rmsnorm_generic_kernel(const __hip_bfloat16* __restrict__ x,
                                                              const __hip_bfloat16* __restrict__ g,
                                                              __hip_bfloat16* __restrict__ out, int rows, int d) {
    const int row = blockIdx.x * 4 + (threadIdx.x >> 6);
    const int lane = threadIdx.x & 63;
    if (row >= rows) return;
    const __hip_bfloat16* xr = x + (size_t)row * d;
    float ss = 0.f;
    for (int c = lane; c < d; c += 64) {
        const float v = __bfloat162float(xr[c]);
        ss = fmaf(v, v, ss);
    }
    for (int o = 32; o > 0; o >>= 1) ss += __shfl_xor(ss, o, 64);
    const float inv = rsqrtf(ss / (float)d + 1e-6f);
    for (int c = lane; c < d; c += 64)
        out[(size_t)row * d + c] = __float2bfloat16(__bfloat162float(xr[c]) * inv * __bfloat162float(g[c]));
}

extern "C" void kernel_launch(void* const* d_in, const int* in_sizes, int n_in, void* d_out, int out_size,
                              void* d_ws, size_t ws_size, hipStream_t stream) {
    const int d = in_sizes[1];
    const int rows = in_sizes[0] / d;
    if (d == RN_D && rows == RN_ROWS) {
        rmsnorm448_kernel<<<RN_ROWS / 16, 256, 0, stream>>>(d_in[0], d_in[1], d_out);
    } else {
        rmsnorm_generic_kernel<<<(rows + 3) / 4, 256, 0, stream>>>((const __hip_bfloat16*)d_in[0],
                                                                  (const __hip_bfloat16*)d_in[1],
                                                                  (__hip_bfloat16*)d_out, rows, d);
    }
}
```

```cpp
#include <hip/hip_runtime.h>
#include <hip/hip_bf16.h>
#include <stdint.h>

#define RN_ROWS 16384
#define RN_D 448

#define UNPK(dlo, dhi, src)                         \
    "v_lshlrev_b32 v" #dlo ", 16, v" #src "\n\t"    \
    "v_and_b32 v" #dhi ", s22, v" #src "\n\t"
#define MULG(d, a) "v_pk_mul_f32 v[" #d "], v[" #a "], v[" #d "]\n\t"
#define MULI(d) "v_pk_mul_f32 v[" #d "], v[2:3], v[" #d "] op_sel_hi:[0,1]\n\t"
#define PACK(d, a, b) "v_cvt_pk_bf16_f32 v" #d ", v" #a ", v" #b "\n\t"
#define DOT(acc, r) "v_dot2c_f32_bf16 v" #acc ", v" #r ", v" #r "\n\t"
#define DPPADD(ctrl) "v_add_f32_dpp v2, v2, v2 " ctrl " row_mask:0xf bank_mask:0xf bound_ctrl:1\n\t"

__global__ __launch_bounds__(256) void rmsnorm448_kernel(const void* __restrict__ x, const void* __restrict__ g,
                                                         void* __restrict__ out) {
    __shared__ uint4 gs[256];
    asm volatile(
        "s_load_dwordx2 s[4:5], %0, 0x0\n\t"
        "s_load_dwordx2 s[8:9], %0, 0x8\n\t"
        "s_load_dwordx2 s[12:13], %0, 0x10\n\t"
        "s_mov_b32 s22, 0xffff0000\n\t"
        "s_mov_b32 s6, 0xe00000\n\t"
        "s_mov_b32 s7, 0x20000\n\t"
        "s_movk_i32 s10, 0x380\n\t"
        "s_mov_b32 s11, s7\n\t"
        "s_mov_b32 s14, s6\n\t"
        "s_mov_b32 s15, s7\n\t"
        "s_mul_i32 s18, %1, 14336\n\t"
        "s_movk_i32 s19, 0x100\n\t"
        "v_lshrrev_b32 v1, 4, %2\n\t"
        "v_and_b32 v2, 15, %2\n\t"
        "v_lshlrev_b32 v3, 4, v2\n\t"
        "v_mad_u32_u24 v4, v1, s10, v3\n\t"
        "v_add_u32 v4, s18, v4\n\t"
        "v_bfrev_b32 v5, 1\n\t"
        "v_cmp_gt_u32 vcc, 8, v2\n\t"
        "v_lshlrev_b32 v7, 4, %2\n\t"
        "v_and_b32 v6, 0x3f0, v7\n\t"
        "v_cndmask_b32 v5, v5, v4, vcc\n\t"
        "v_and_b32 v1, 0xc00, v7\n\t"
        "v_add_u32 v7, %3, v7\n\t"
        "v_or_b32 v1, v1, v3\n\t"
        "v_add_u32 v1, %3, v1\n\t"
        "v_mov_b32 v2, 0\n\t"
        "v_mov_b32 v3, 0\n\t"
        "v_mov_b32 v72, 0x358637bd\n\t"
        "s_waitcnt lgkmcnt(0)\n\t"
        "s_and_b32 s9, s9, 0xffff\n\t"
        "s_and_b32 s5, s5, 0xffff\n\t"
        "buffer_load_dwordx4 v[24:27], v6, s[8:11], 0 offen\n\t"
        "buffer_load_dwordx4 v[8:11], v4, s[4:7], 0 offen nt\n\t"
        "buffer_load_dwordx4 v[12:15], v4, s[4:7], 0 offen offset:256 nt\n\t"
        "buffer_load_dwordx4 v[16:19], v4, s[4:7], 0 offen offset:512 nt\n\t"
        "buffer_load_dwordx4 v[20:23], v5, s[4:7], 0 offen offset:768 nt\n\t"
        "s_memrealtime s[16:17]\n\t"
        "s_and_b32 s13, s13, 0xffff\n\t"
        "s_waitcnt vmcnt(4)\n\t"
        "ds_write_b128 v7, v[24:27]\n\t"
        "ds_read_b128 v[24:27], v1\n\t"
        "ds_read_b128 v[28:31], v1 offset:256\n\t"
        "ds_read_b128 v[32:35], v1 offset:512\n\t"
        "ds_read_b128 v[36:39], v1 offset:768\n\t"
        "s_waitcnt lgkmcnt(0)\n\t"
        UNPK(40, 41, 24) UNPK(42, 43, 25) UNPK(44, 45, 26) UNPK(46, 47, 27)
        UNPK(48, 49, 28) UNPK(50, 51, 29) UNPK(52, 53, 30) UNPK(54, 55, 31)
        UNPK(56, 57, 32) UNPK(58, 59, 33) UNPK(60, 61, 34) UNPK(62, 63, 35)
        UNPK(64, 65, 36) UNPK(66, 67, 37) UNPK(68, 69, 38) UNPK(70, 71, 39)
        "s_waitcnt vmcnt(3)\n\t"
        DOT(2, 8) DOT(3, 9) DOT(2, 10) DOT(3, 11)
        UNPK(24, 25, 8) UNPK(26, 27, 9) UNPK(28, 29, 10) UNPK(30, 31, 11)
        MULG(40:41, 24:25) MULG(42:43, 26:27) MULG(44:45, 28:29) MULG(46:47, 30:31)
        "s_waitcnt vmcnt(2)\n\t"
        DOT(2, 12) DOT(3, 13) DOT(2, 14) DOT(3, 15)
        UNPK(32, 33, 12) UNPK(34, 35, 13) UNPK(36, 37, 14) UNPK(38, 39, 15)
        MULG(48:49, 32:33) MULG(50:51, 34:35) MULG(52:53, 36:37) MULG(54:55, 38:39)
        "s_waitcnt vmcnt(1)\n\t"
        DOT(2, 16) DOT(3, 17) DOT(2, 18) DOT(3, 19)
        "s_waitcnt vmcnt(0)\n\t"
        "s_memrealtime s[20:21]\n\t"
        DOT(2, 20) DOT(3, 21) DOT(2, 22) DOT(3, 23)
        UNPK(24, 25, 16) UNPK(26, 27, 17)
        "v_add_f32 v2, v2, v3\n\t"
        UNPK(28, 29, 18)
        DPPADD("quad_perm:[1,0,3,2]")
        UNPK(30, 31, 19)
        DPPADD("quad_perm:[2,3,0,1]")
        MULG(56:57, 24:25) MULG(58:59, 26:27)
        DPPADD("row_half_mirror")
        MULG(60:61, 28:29) MULG(62:63, 30:31)
        DPPADD("row_mirror")
        UNPK(32, 33, 20)
        "v_fmac_f32 v72, 0x3b124925, v2\n\t"
        UNPK(34, 35, 21)
        "v_rsq_f32 v2, v72\n\t"
        UNPK(36, 37, 22) UNPK(38, 39, 23)
        MULG(64:65, 32:33) MULG(66:67, 34:35) MULG(68:69, 36:37) MULG(70:71, 38:39)
        MULI(40:41) MULI(42:43) MULI(44:45) MULI(46:47)
        MULI(48:49) MULI(50:51) MULI(52:53) MULI(54:55)
        PACK(8, 40, 41) PACK(9, 42, 43) PACK(10, 44, 45) PACK(11, 46, 47)
        MULI(56:57) MULI(58:59) MULI(60:61) MULI(62:63)
        PACK(12, 48, 49) PACK(13, 50, 51) PACK(14, 52, 53) PACK(15, 54, 55)
        MULI(64:65) MULI(66:67) MULI(68:69) MULI(70:71)
        PACK(16, 56, 57) PACK(17, 58, 59) PACK(18, 60, 61) PACK(19, 62, 63)
        PACK(20, 64, 65) PACK(21, 66, 67) PACK(22, 68, 69) PACK(23, 70, 71)
        "1:\n\t"
        "s_waitcnt lgkmcnt(0)\n\t"
        "s_sub_u32 s20, s20, s16\n\t"
        "s_cmp_ge_u32 s20, 210\n\t"
        "s_cbranch_scc1 2f\n\t"
        "s_sleep 1\n\t"
        "s_memrealtime s[20:21]\n\t"
        "s_sub_u32 s19, s19, 1\n\t"
        "s_cbranch_scc0 1b\n\t"
        "2:\n\t"
        "buffer_store_dwordx4 v[8:11], v4, s[12:15], 0 offen nt\n\t"
        "buffer_store_dwordx4 v[12:15], v4, s[12:15], 0 offen offset:256 nt\n\t"
        "buffer_store_dwordx4 v[16:19], v4, s[12:15], 0 offen offset:512 nt\n\t"
        "buffer_store_dwordx4 v[20:23], v5, s[12:15], 0 offen offset:768 nt\n\t"
        :
        : "s"(__builtin_amdgcn_kernarg_segment_ptr()), "s"(__builtin_amdgcn_workgroup_id_x()),
          "v"(__builtin_amdgcn_workitem_id_x()), "s"((unsigned)(uintptr_t)gs)
        : "memory", "vcc", "scc", "s4", "s5", "s6", "s7", "s8", "s9", "s10", "s11", "s12", "s13", "s14", "s15", "s16",
          "s17", "s18", "s19", "s20", "s21", "s22", "v1", "v2", "v3", "v4", "v5", "v6", "v7", "v8", "v9", "v10", "v11",
          "v12", "v13", "v14", "v15", "v16", "v17", "v18", "v19", "v20", "v21", "v22", "v23", "v24", "v25", "v26",
          "v27", "v28", "v29", "v30", "v31", "v32", "v33", "v34", "v35", "v36", "v37", "v38", "v39", "v40", "v41",
          "v42", "v43", "v44", "v45", "v46", "v47", "v48", "v49", "v50", "v51", "v52", "v53", "v54", "v55", "v56",
          "v57", "v58", "v59", "v60", "v61", "v62", "v63", "v64", "v65", "v66", "v67", "v68", "v69", "v70", "v71",
          "v72");
}

__global__ __launch_bounds__(256) void rmsnorm_generic_kernel(const __hip_bfloat16* __restrict__ x,
                                                              const __hip_bfloat16* __restrict__ g,
                                                              __hip_bfloat16* __restrict__ out, int rows, int d) {
    const int row = blockIdx.x * 4 + (threadIdx.x >> 6);
    const int lane = threadIdx.x & 63;
    if (row >= rows) return;
    const __hip_bfloat16* xr = x + (size_t)row * d;
    float ss = 0.f;
    for (int c = lane; c < d; c += 64) {
        const float v = __bfloat162float(xr[c]);
        ss = fmaf(v, v, ss);
    }
    for (int o = 32; o > 0; o >>= 1) ss += __shfl_xor(ss, o, 64);
    const float inv = rsqrtf(ss / (float)d + 1e-6f);
    for (int c = lane; c < d; c += 64)
        out[(size_t)row * d + c] = __float2bfloat16(__bfloat162float(xr[c]) * inv * __bfloat162float(g[c]));
}

extern "C" void kernel_launch(void* const* d_in, const int* in_sizes, int n_in, void* d_out, int out_size,
                              void* d_ws, size_t ws_size, hipStream_t stream) {
    const int d = in_sizes[1];
    const int rows = in_sizes[0] / d;
    if (d == RN_D && rows == RN_ROWS) {
        rmsnorm448_kernel<<<RN_ROWS / 16, 256, 0, stream>>>(d_in[0], d_in[1], d_out);
    } else {
        rmsnorm_generic_kernel<<<(rows + 3) / 4, 256, 0, stream>>>((const __hip_bfloat16*)d_in[0],
                                                                  (const __hip_bfloat16*)d_in[1],
                                                                  (__hip_bfloat16*)d_out, rows, d);
    }
}
```
